```python
import torch

def _softmax(x, axis=-1):
    return torch.softmax(x, dim=axis)
def _gelu(x):
    return torch.nn.functional.gelu(x, approximate="tanh")
def _silu(x):
    return torch.nn.functional.silu(x)
def _sigmoid(x):
    return torch.sigmoid(x)
def _layernorm(x, axis=-1, eps=1e-5):
    m = x.mean(dim=axis, keepdim=True)
    v = x.var(dim=axis, keepdim=True, unbiased=False)
    return (x - m) / torch.sqrt(v + eps)
def _scatter_add(tgt, idx, src, axis=0):
    return tgt.clone().index_add_(axis, idx, src)
def _segment_sum(src, seg, n):
    out = torch.zeros((n,) + src.shape[1:], dtype=src.dtype)
    return out.index_add_(0, seg, src)
def _pad(x, pw):
    flat = tuple(v for pair in reversed(pw) for v in pair)
    return torch.nn.functional.pad(x, flat)

kernel_name = 'recurrent_scan__sliding_block_cached__dense_activation_relu__prompt'

def setup_inputs():

    _w = (lambda a: (lambda t: t.to(torch.bfloat16) if t.is_floating_point() else t)(torch.as_tensor(a)))
    def _A_setup():
        B, Sq, D = 1, 2048, 1024
        Hq, Hkv, Sk, Dh = 32, 4, 2048, 128
        assert Hq % Hkv == 0, (Hq, Hkv)
        return {
            "x": _w((lambda *s: torch.randn(*s, dtype=torch.bfloat16) * 0.05)(B, Sq, D)),
            "k_cache": _w((lambda *s: torch.randn(*s, dtype=torch.bfloat16) * 0.8)(B, Hkv, Sk, Dh)),
            "v_cache": _w((lambda *s: torch.randn(*s, dtype=torch.bfloat16) * 0.2)(B, Hkv, Sk, Dh)),
            "wq": _w((lambda *s: torch.randn(*s, dtype=torch.bfloat16) * 0.8)(D, Hq * Dh) * (D ** -0.5)),
            "wo": _w((lambda *s: torch.randn(*s, dtype=torch.bfloat16) * 0.2)(Hq * Dh, D) * ((Hq * Dh) ** -0.5)),
        }
    _A_in = _A_setup()

    def _B_setup():
        B, S, D, Dff = 1, 2048, 1024, 3584
        return {
            "x": _w((lambda *s: torch.randn(*s, dtype=torch.bfloat16) * 0.05)(B, S, D)),
            "w1": _w((lambda *s: torch.randn(*s, dtype=torch.bfloat16) * 0.1)(D, Dff)),
            "w2": _w((lambda *s: torch.randn(*s, dtype=torch.bfloat16) * 0.1)(Dff, D)),
        }
    _B_in = _B_setup()

    return {
        "A_x": _A_in["x"],
        "A_k_cache": _A_in["k_cache"],
        "A_v_cache": _A_in["v_cache"],
        "A_wq": _A_in["wq"],
        "A_wo": _A_in["wo"],
        "B_x": _B_in["x"],
        "B_w1": _B_in["w1"],
        "B_w2": _B_in["w2"],
    }

def reference(A_x, A_k_cache, A_v_cache, A_wq, A_wo, B_x, B_w1, B_w2):
    def _A_ref(x, k_cache, v_cache, wq, wo):
        B, Sq, D = x.shape
        Hq, Dh = 32, 128
        Hkv, Sk = k_cache.shape[1], k_cache.shape[2]
        W = min(4096, max(256, Sk // 4))
        rep = Hq // Hkv
        q = (lambda x, *d: x.permute(*d))((x @ wq).reshape(B, Sq, Hq, Dh), 0, 2, 1, 3)
        k = (lambda x, n, axis: torch.repeat_interleave(x, n, dim=axis))(k_cache[:, :, Sk - W:, :], rep, axis=1)
        v = (lambda x, n, axis: torch.repeat_interleave(x, n, dim=axis))(v_cache[:, :, Sk - W:, :], rep, axis=1)
        scores = torch.einsum("bhqd,bhkd->bhqk", q, k) * (Dh ** -0.5)
        p = _softmax(scores, axis=-1)
        o = torch.einsum("bhqk,bhkd->bhqd", p, v)
        return (lambda x, *d: x.permute(*d))(o, 0, 2, 1, 3).reshape(B, Sq, Hq * Dh) @ wo

    def _B_ref(x, w1, w2):
        h = torch.einsum("bsd,df->bsf", x, w1)
        zero = torch.zeros_like(h)
        h = torch.maximum(h, zero)
        return torch.einsum("bsf,fd->bsd", h, w2)

    x = A_x
    xn = _layernorm(x, axis=-1)
    s = _A_ref(xn, A_k_cache, A_v_cache, A_wq, A_wo)
    g = _silu(_B_ref(xn, B_w1, B_w2))
    return x + s * g
```

```cpp
#include <hip/hip_runtime.h>
#include <hip/hip_bf16.h>
#include <stdint.h>

typedef unsigned short u16;
typedef short bf16x8 __attribute__((ext_vector_type(8)));
typedef short bf16x4 __attribute__((ext_vector_type(4)));
typedef float f32x4 __attribute__((ext_vector_type(4)));
typedef float f32x16 __attribute__((ext_vector_type(16)));
typedef unsigned u32x4 __attribute__((ext_vector_type(4)));
typedef unsigned u32x2 __attribute__((ext_vector_type(2)));
#define LAS __attribute__((address_space(3)))

constexpr int SEQ = 2048, DM = 1024, NQ = 4096, DFF = 3584, NH = 32, NKV = 4, DH = 128, SK = 2048, WIN = 512;

constexpr size_t WS_XN = 0;
constexpr size_t WS_Q  = WS_XN + (size_t)SEQ * DM * 2;
constexpr size_t WS_H  = WS_Q + (size_t)SEQ * NQ * 2;
constexpr size_t WS_O  = WS_H + (size_t)SEQ * DFF * 2;
constexpr size_t WS_S  = WS_O + (size_t)SEQ * NQ * 2;
constexpr size_t WS_G  = WS_S + (size_t)SEQ * DM * 2;
constexpr size_t WS_END = WS_G + (size_t)SEQ * DM * 2;
constexpr size_t WS_STAMP = (size_t)200 << 20;

#define TSTAMP(i) do {} while (0)
#define TSTAMP_DECL do {} while (0)
#define TSTAMP_FLUSH do {} while (0)
#define CSTAMP(kid, i) do {} while (0)
#define CSTAMP_DECL do {} while (0)
#define CSTAMP_FLUSH(kid) do {} while (0)
#define STAMP(i) do {} while (0)
#define KSTAMP(kid, i) do {} while (0)
__device__ __forceinline__ float bf2f(u16 h) { return __uint_as_float((unsigned)h << 16); }
__device__ __forceinline__ unsigned pk2bf(float lo, float hi) {
  unsigned r; asm volatile("v_cvt_pk_bf16_f32 %0, %1, %2" : "=v"(r) : "v"(lo), "v"(hi)); return r;
}
__device__ __forceinline__ float rbf(float f) { return __uint_as_float(pk2bf(f, f) << 16); }
__device__ __forceinline__ float silu_f(float a) {
  return a * __builtin_amdgcn_rcpf(1.f + __builtin_amdgcn_exp2f(a * -1.4426950408889634f));
}

__global__ __launch_bounds__(256) void ln_kernel(const u16* __restrict__ x, u16* __restrict__ xn, unsigned* __restrict__ sync, unsigned long long* stamp_buf) {
  KSTAMP(0, 0);
  if (blockIdx.x < 32) sync[blockIdx.x * 256 + threadIdx.x] = 0u;
  const int lane = threadIdx.x & 63, row = blockIdx.x * 4 + (threadIdx.x >> 6);
  const u16* xr = x + (size_t)row * DM;
  const bf16x8 a = *reinterpret_cast<const bf16x8*>(xr + lane * 8), b = *reinterpret_cast<const bf16x8*>(xr + 512 + lane * 8);
  float v[16];
#pragma unroll
  for (int i = 0; i < 8; ++i) { v[i] = bf2f((u16)a[i]); v[8 + i] = bf2f((u16)b[i]); }
  float s = 0.f;
#pragma unroll
  for (int i = 0; i < 16; ++i) s += v[i];
#pragma unroll
  for (int o = 32; o > 0; o >>= 1) s += __shfl_xor(s, o);
  const float mean = s * (1.0f / DM);
  float q = 0.f;
#pragma unroll
  for (int i = 0; i < 16; ++i) { const float d = v[i] - mean; q += d * d; }
#pragma unroll
  for (int o = 32; o > 0; o >>= 1) q += __shfl_xor(q, o);
  const float mb = rbf(mean), vb = rbf(q * (1.0f / DM));
  const float den = rbf(sqrtf(rbf(vb + 1e-5f)));
  u32x4 w0, w1;
#pragma unroll
  for (int i = 0; i < 4; ++i) {
    w0[i] = pk2bf(rbf(v[2 * i] - mb) / den, rbf(v[2 * i + 1] - mb) / den);
    w1[i] = pk2bf(rbf(v[8 + 2 * i] - mb) / den, rbf(v[8 + 2 * i + 1] - mb) / den);
  }
  u16* o = xn + (size_t)row * DM;
  *reinterpret_cast<u32x4*>(o + lane * 8) = w0;
  *reinterpret_cast<u32x4*>(o + 512 + lane * 8) = w1;
  KSTAMP(0, 1);
}

constexpr int G_HTB = 16384, G_SHM = 8 * G_HTB;

__device__ __forceinline__ void glds16(const void* g, LAS char* l) {
  __builtin_amdgcn_global_load_lds((const unsigned*)g, (LAS unsigned*)l, 16, 0, 0);
}
__device__ __forceinline__ bf16x8 ld_a(const LAS char* p) { return *reinterpret_cast<const LAS bf16x8*>(p); }
template <int OFF> __device__ __forceinline__ bf16x8 ld_w(int addr) {
  bf16x4 lo, hi;
  asm volatile("ds_read_b64_tr_b16 %0, %1 offset:%2" : "=&v"(lo) : "v"(addr), "i"(OFF) : "memory");
  asm volatile("ds_read_b64_tr_b16 %0, %1 offset:%2" : "=&v"(hi) : "v"(addr), "i"(OFF + 1024) : "memory");
  return (bf16x8){lo[0], lo[1], lo[2], lo[3], hi[0], hi[1], hi[2], hi[3]};
}

struct GemmArgs { const u16* A; const u16* W; u16* C; int lda, ldw, ldc, K, relu; };

__device__ __forceinline__ void gemm256_tile(const GemmArgs& p, int brow, int bcol, int ccol, LAS char* shm, unsigned long long* stamp_buf) {
  const int tid = threadIdx.x;
  CSTAMP_DECL;
  const u16* __restrict__ A = p.A; const u16* __restrict__ Wt = p.W; const int lda = p.lda, ldw = p.ldw;
  unsigned aoff[2], woff[2];
  const int wid = tid >> 6, lane = tid & 63, wr = wid >> 2, wc = wid & 3, fr = lane & 15, fq = lane >> 4;
#pragma unroll
  for (int i = 0; i < 2; ++i) {
    const int r8 = lane >> 3, cp8 = lane & 7, sA = (i << 2) | (r8 >> 1);
    aoff[i] = (unsigned)((brow + (wid * 2 + i) * 8 + r8) * lda + 8 * (cp8 ^ sA));
    const int b = tid * 16 + i * 8192;
    const int k = b >> 8, cp = (b >> 4) & 15, f = (k & 3) | (((k >> 3) & 1) << 2);
    woff[i] = (unsigned)(k * ldw + bcol + 8 * (cp ^ (f << 1)));
  }
#define SA(b, h) (shm + ((b) * 2 + (h)) * G_HTB)
#define SB(b, h) (shm + (4 + (b) * 2 + (h)) * G_HTB)
#define STAGE_A(P, h, kt) do { _Pragma("unroll") for (int _i = 0; _i < 2; ++_i) \
    glds16(A + (size_t)aoff[_i] + (size_t)((h) * 128) * lda + (kt) * 64, (P) + wid * 2048 + lane * 16 + _i * 1024); } while (0)
#define STAGE_B(P, h, kt) do { _Pragma("unroll") for (int _i = 0; _i < 2; ++_i) \
    glds16(Wt + (size_t)woff[_i] + (h) * 128 + (size_t)((kt) * 64) * ldw, (P) + tid * 16 + _i * 8192); } while (0)
  const int a_s = (fr >> 1) & 7, a_row = wr * 8192 + (fr >> 3) * 1024 + (fr & 7) * 128;
  const int a_base0 = a_row + ((fq ^ a_s) << 4), a_base1 = a_row + (((4 + fq) ^ a_s) << 4);
  const int tq = (lane >> 2) & 3, tp = lane & 3, tf = tq | ((fq & 1) << 2);
  const int w_base0 = (int)(uintptr_t)shm + 4 * G_HTB + (8 * fq + tq) * 256 + ((wc * 64 + 8 * tp) ^ (tf << 5));
  const int w_base1 = (int)(uintptr_t)shm + 4 * G_HTB + (8 * fq + tq) * 256 + ((wc * 64 + 32 + 8 * tp) ^ (tf << 5));
#define LDA(dst, b, h) _Pragma("unroll") for (int m = 0; m < 4; ++m) { \
    dst[m][0] = ld_a(SA(b, h) + a_base0 + m * 2048); dst[m][1] = ld_a(SA(b, h) + a_base1 + m * 2048); }
#define LDB(dst, b, h) do { dst[0][0] = ld_w<((b) * 2 + (h)) * G_HTB>(w_base0); dst[1][0] = ld_w<((b) * 2 + (h)) * G_HTB>(w_base1); \
    dst[0][1] = ld_w<((b) * 2 + (h)) * G_HTB + 8192>(w_base0); dst[1][1] = ld_w<((b) * 2 + (h)) * G_HTB + 8192>(w_base1); } while (0)
#define MMA(ai, bj, At, Bt) do { __builtin_amdgcn_s_setprio(1); \
    _Pragma("unroll") for (int m = 0; m < 4; ++m) _Pragma("unroll") for (int n = 0; n < 2; ++n) _Pragma("unroll") for (int k = 0; k < 2; ++k) \
      acc[ai][bj][m][n] = __builtin_amdgcn_mfma_f32_16x16x32_bf16(Bt[n][k], At[m][k], acc[ai][bj][m][n], 0, 0, 0); \
    __builtin_amdgcn_s_setprio(0); } while (0)
#define WAIT_V(n) asm volatile("s_waitcnt vmcnt(" #n ")" ::: "memory")
#define WAIT_L(n) asm volatile("s_waitcnt lgkmcnt(" #n ")" ::: "memory")
#define WL0 do { asm volatile("s_waitcnt lgkmcnt(0)" ::: "memory"); __builtin_amdgcn_sched_barrier(0); } while (0)
#define BAR __builtin_amdgcn_s_barrier()
#define SCHED __builtin_amdgcn_sched_barrier(0)
  f32x4 acc[2][2][4][2] = {};
  bf16x8 At[4][2], B0[2][2], B1[2][2];
  const int nt = p.K / 64;
  STAGE_B(SB(0, 0), 0, 0); STAGE_A(SA(0, 0), 0, 0);
  STAGE_B(SB(0, 1), 1, 0); STAGE_A(SA(0, 1), 1, 0);
  if (wr == 1) BAR;
  WAIT_V(4); BAR;
  STAGE_B(SB(1, 0), 0, 1); STAGE_A(SA(1, 0), 0, 1); STAGE_B(SB(1, 1), 1, 1);
  WAIT_V(6); BAR;
  CSTAMP(1, 0);
  for (int t = 0; t < nt - 2; t += 2) {
    LDB(B0, 0, 0); SCHED; LDA(At, 0, 0); STAGE_A(SA(1, 1), 1, t + 1);
    WAIT_L(8); BAR; WL0; MMA(0, 0, At, B0); BAR; SCHED;
    LDB(B1, 0, 1); STAGE_B(SB(0, 0), 0, t + 2);
    BAR; WL0; MMA(0, 1, At, B1); BAR;
    LDA(At, 0, 1); STAGE_A(SA(0, 0), 0, t + 2);
    BAR; WL0; MMA(1, 0, At, B0); BAR; SCHED;
    STAGE_B(SB(0, 1), 1, t + 2);
    WAIT_V(6); BAR; MMA(1, 1, At, B1); BAR;
    LDB(B0, 1, 0); SCHED; LDA(At, 1, 0); STAGE_A(SA(0, 1), 1, t + 2);
    WAIT_L(8); BAR; WL0; MMA(0, 0, At, B0); BAR; SCHED;
    LDB(B1, 1, 1); STAGE_B(SB(1, 0), 0, t + 3);
    BAR; WL0; MMA(0, 1, At, B1); BAR;
    LDA(At, 1, 1); STAGE_A(SA(1, 0), 0, t + 3);
    BAR; WL0; MMA(1, 0, At, B0); BAR; SCHED;
    STAGE_B(SB(1, 1), 1, t + 3);
    WAIT_V(6); BAR; MMA(1, 1, At, B1); BAR;
  }
  { LDB(B0, 0, 0); WAIT_V(0); LDA(At, 0, 0); STAGE_A(SA(1, 1), 1, nt - 1);
    BAR; WL0; MMA(0, 0, At, B0); BAR;
    LDB(B1, 0, 1); BAR; WL0; MMA(0, 1, At, B1); BAR;
    LDA(At, 0, 1); WAIT_V(4); BAR; WL0; MMA(1, 0, At, B0); MMA(1, 1, At, B1); BAR; }
  { LDB(B0, 1, 0); LDA(At, 1, 0); WAIT_V(2); BAR; WL0; MMA(0, 0, At, B0); BAR;
    LDB(B1, 1, 1); WAIT_V(0); BAR; WL0; MMA(0, 1, At, B1); BAR;
    LDA(At, 1, 1); BAR; WL0; MMA(1, 0, At, B0); MMA(1, 1, At, B1); BAR; }
  if (wr == 0) BAR;
  CSTAMP(1, 1);
  u16* __restrict__ Cp = p.C + (size_t)(brow + wr * 64 + fr) * p.ldc + ccol + wc * 32 + (fq & 1) * 16 + (fq >> 1) * 8;
  const bool relu = p.relu;
#pragma unroll
  for (int ai = 0; ai < 2; ++ai)
#pragma unroll
    for (int m = 0; m < 4; ++m)
#pragma unroll
      for (int bj = 0; bj < 2; ++bj) {
        f32x4 v0 = acc[ai][bj][m][0], v1 = acc[ai][bj][m][1];
        if (relu) {
#pragma unroll
          for (int j = 0; j < 4; ++j) { v0[j] = fmaxf(v0[j], 0.f); v1[j] = fmaxf(v1[j], 0.f); }
        }
        const unsigned a0 = pk2bf(v0[0], v0[1]), a1 = pk2bf(v0[2], v0[3]), b0 = pk2bf(v1[0], v1[1]), b1 = pk2bf(v1[2], v1[3]);
        const auto r0 = __builtin_amdgcn_permlane16_swap(a0, b0, false, false);
        const auto r1 = __builtin_amdgcn_permlane16_swap(a1, b1, false, false);
        const u32x4 w = {r0[0], r1[0], r0[1], r1[1]};
        *reinterpret_cast<u32x4*>(Cp + (size_t)(ai * 128 + m * 16) * p.ldc + bj * 128) = w;
      }
  CSTAMP_FLUSH(1);
#undef SA
#undef SB
#undef STAGE_A
#undef STAGE_B
#undef LDA
#undef LDB
#undef MMA
}

__global__ __launch_bounds__(512, 2) void gemm1_kernel(const u16* __restrict__ xn, const u16* __restrict__ wq, const u16* __restrict__ w1,
                                                      u16* __restrict__ Q, u16* __restrict__ H, unsigned long long* stamp_buf) {
  extern __shared__ __attribute__((aligned(16))) char shm_[];
  LAS char* shm = (LAS char*)shm_;
  KSTAMP(1, 0);
  const int bid = blockIdx.x, w = (bid & 7) * 30 + (bid >> 3), pm = w & 7, pn = w >> 3;
  GemmArgs p; p.A = xn; p.lda = DM; p.K = DM;
  int bcol;
  if (pn < 16) { p.W = wq; p.ldw = NQ; p.C = Q; p.ldc = NQ; p.relu = 0; bcol = pn * 256; }
  else { p.W = w1; p.ldw = DFF; p.C = H; p.ldc = DFF; p.relu = 1; bcol = (pn - 16) * 256; }
  gemm256_tile(p, pm * 256, bcol, bcol, shm, stamp_buf);
  KSTAMP(1, 1);
}

constexpr int G3_SHM = 163840;
constexpr size_t WS_XCH = WS_S;
constexpr size_t WS_SYNC = WS_G + (size_t)SEQ * DM * 2;

__global__ __launch_bounds__(512, 2) void gemm3_kernel(const u16* __restrict__ O, const u16* __restrict__ H, const u16* __restrict__ wo, const u16* __restrict__ w2,
                                                      const u16* __restrict__ x, u16* __restrict__ out, u16* __restrict__ xch, unsigned* __restrict__ sync,
                                                      unsigned long long* stamp_buf) {
  extern __shared__ __attribute__((aligned(16))) char shm_[];
  LAS char* shm = (LAS char*)shm_;
  KSTAMP(3, 0);
  TSTAMP_DECL; CSTAMP_DECL;
  const int bid = blockIdx.x, xcd = bid & 7, idx = bid >> 3;
  const int role = idx & 1, pm = (xcd & 3) * 4 + ((idx >> 1) & 3), pn = (xcd >> 2) * 4 + (idx >> 3);
  const u16* __restrict__ A = role ? H : O; const u16* __restrict__ Wt = role ? w2 : wo;
  const int lda = role ? DFF : NQ, nph = lda >> 6, brow = pm * 128, bcol = pn * 128;
  const int tid = threadIdx.x, wid = __builtin_amdgcn_readfirstlane(tid >> 6), lane = tid & 63, grp = wid >> 2, w4 = wid & 3, wr = w4 >> 1, wc = w4 & 1, fr = lane & 15, fq = lane >> 4;
  const int tq = (lane >> 2) & 3, tp = lane & 3, tf = tq | ((fq & 1) << 2);
  unsigned aoff[2], woff[2];
#pragma unroll
  for (int i = 0; i < 2; ++i) {
    const int r8 = lane >> 3, cp8 = lane & 7, sA = (i << 2) | (r8 >> 1);
    aoff[i] = (unsigned)((brow + (w4 * 2 + i) * 8 + r8) * lda + grp * 64 + 8 * (cp8 ^ sA));
    const int k = (w4 * 2 + i) * 4 + (lane >> 4), cp = lane & 15, f = (k & 3) | (((k >> 3) & 1) << 2);
    woff[i] = (unsigned)((grp * 64 + k) * DM + bcol + 8 * (cp ^ (f << 1)));
  }
  LAS char* gA = shm + grp * 81920; LAS char* gW = gA + 49152;
  const int stg = w4 * 2048;
  const int nblk = nph >> 1;
#define STAGE_A3(sa, b, h) do { const size_t o_ = (size_t)((h) * 64) * lda + (b) * 128; \
    glds16(A + (size_t)aoff[0] + o_, gA + (sa) + (h) * 8192 + stg); glds16(A + (size_t)aoff[1] + o_, gA + (sa) + (h) * 8192 + stg + 1024); } while (0)
#define STAGE_W3(ws, c) do { const size_t o_ = (size_t)(((c) >> 1) * 128 + ((c) & 1) * 32) * DM; \
    glds16(Wt + (size_t)woff[0] + o_, gW + (ws) * 8192 + stg); glds16(Wt + (size_t)woff[1] + o_, gW + (ws) * 8192 + stg + 1024); } while (0)
  const int a_s = (fr >> 1) & 7, a_row = grp * 81920 + wr * 8192 + (fr >> 3) * 1024 + (fr & 7) * 128;
  const int a_base0 = a_row + ((fq ^ a_s) << 4), a_base1 = a_row + (((4 + fq) ^ a_s) << 4);
  int w_base[4];
#pragma unroll
  for (int n = 0; n < 4; ++n) w_base[n] = (int)(uintptr_t)shm + grp * 81920 + 49152 + (8 * fq + tq) * 256 + ((((wc << 2) + n) ^ tf) << 5) + 8 * tp;
  f32x4 acc[4][4] = {};
  bf16x8 At[4], Bt[4];
#define MMA3() do { __builtin_amdgcn_s_setprio(1); \
    _Pragma("unroll") for (int m = 0; m < 4; ++m) _Pragma("unroll") for (int n = 0; n < 4; ++n) \
      acc[m][n] = __builtin_amdgcn_mfma_f32_16x16x32_bf16(Bt[n], At[m], acc[m][n], 0, 0, 0); \
    __builtin_amdgcn_s_setprio(0); } while (0)
#define PHASE(WS, KS, sa_rd, STG, WAITS) do { \
    Bt[0] = ld_w<(WS) * 8192>(w_base[0]); Bt[1] = ld_w<(WS) * 8192>(w_base[1]); Bt[2] = ld_w<(WS) * 8192>(w_base[2]); Bt[3] = ld_w<(WS) * 8192>(w_base[3]); \
    { const LAS char* ap_ = shm + ((KS) ? a_base1 : a_base0) + (sa_rd); _Pragma("unroll") for (int m = 0; m < 4; ++m) At[m] = ld_a(ap_ + m * 2048); } \
    STG; BAR; WL0; MMA3(); WAITS; BAR; } while (0)
  const unsigned eoff = (unsigned)((brow + wr * 64 + grp * 32 + fr) * DM + bcol + wc * 64 + (fq & 1) * 16 + (fq >> 1) * 8);
  STAGE_A3(0, 0, 0); STAGE_A3(0, 0, 1); STAGE_W3(0, 0); STAGE_A3(16384, 1, 0); STAGE_W3(1, 1); STAGE_A3(16384, 1, 1); STAGE_W3(2, 2);
  u32x4 xv[4];
#pragma unroll
  for (int q = 0; q < 4; ++q) xv[q] = *reinterpret_cast<const u32x4*>(x + eoff + (q >> 1) * 16 * DM + (q & 1) * 32);
  unsigned tlb0, tlb1;
  asm volatile("global_load_dword %0, %1, %2 sc1" : "=v"(tlb0) : "v"(eoff * 2), "s"(xch) : "memory");
  asm volatile("global_load_dword %0, %1, %2" : "=v"(tlb1) : "v"(eoff * 2), "s"(out) : "memory");
  WAIT_V(14); BAR;
  if (grp == 1) BAR;
  CSTAMP(3, 0);
  const int tile = pm * 8 + pn;
  unsigned* tick = sync + tile * 64 + wid; unsigned* flag = tick + 32;
  unsigned ticket_v = 0;
  int sA0 = 0, sA1 = 16384, sA2 = 32768;
  for (int p = 0; p < nph - 4; p += 4) {
    const int b0 = p >> 1;
    if (p == nph - 16 && lane == 0)
      asm volatile("global_atomic_add %0, %1, %2, %3 sc0\n\ts_nop 1" : "+v"(ticket_v) : "v"(0u), "v"(1u), "s"(tick) : "memory");
    PHASE(0, 0, sA0, STAGE_A3(sA2, b0 + 2, 0); STAGE_W3(3, p + 3), WAIT_V(8));
    PHASE(1, 1, sA0, STAGE_A3(sA2, b0 + 2, 1); STAGE_W3(0, p + 4), WAIT_V(8));
    PHASE(2, 0, sA1, STAGE_A3(sA0, b0 + 3, 0); STAGE_W3(1, p + 5), WAIT_V(8));
    PHASE(3, 1, sA1, STAGE_A3(sA0, b0 + 3, 1); STAGE_W3(2, p + 6), WAIT_V(8));
    const int t_ = sA0; sA0 = sA2; sA2 = sA1; sA1 = t_;
  }
  PHASE(0, 0, sA0, STAGE_W3(3, nph - 1), WAIT_V(6));
  PHASE(1, 1, sA0, (void)0, WAIT_V(2));
  PHASE(2, 0, sA1, (void)0, WAIT_V(0));
  TSTAMP(0);
  asm volatile("" :: "v"(tlb0), "v"(tlb1));
  const __amdgpu_buffer_rsrc_t rs = __builtin_amdgcn_make_buffer_rsrc(xch, 0, (int)((size_t)SEQ * DM * 2), 0x00020000);
  const unsigned ticket = __builtin_amdgcn_readfirstlane(ticket_v);
  u32x4 oth[4] = {};
  if (ticket != 0u) {
    const unsigned long long t0 = __builtin_amdgcn_s_memrealtime();
    while (__builtin_amdgcn_readfirstlane(__hip_atomic_load(flag, __ATOMIC_RELAXED, __HIP_MEMORY_SCOPE_AGENT)) == 0u) {
      __builtin_amdgcn_s_sleep(1);
      if (__builtin_amdgcn_s_memrealtime() - t0 > 2000000ull) break;
    }
#pragma unroll
    for (int q = 0; q < 4; ++q) oth[q] = __builtin_amdgcn_raw_buffer_load_b128(rs, (eoff + (q >> 1) * 16 * DM + (q & 1) * 32) * 2, 0, 16);
  }
  TSTAMP(1);
  asm volatile("" ::: "memory");
  PHASE(3, 1, sA1, (void)0, (void)0);
  if (grp == 0) BAR;
  CSTAMP(3, 1);
  TSTAMP(2);
  {
    LAS f32x4* R = (LAS f32x4*)shm + (w4 * 8) * 64 + lane;
#pragma unroll
    for (int q = 0; q < 8; ++q) R[grp * 2048 + q * 64] = grp ? acc[q >> 2][q & 3] : acc[2 + (q >> 2)][q & 3];
    WAIT_L(0); BAR; asm volatile("" ::: "memory");
#pragma unroll
    for (int q = 0; q < 8; ++q) { const f32x4 v = R[(grp ^ 1) * 2048 + q * 64]; if (grp) acc[2 + (q >> 2)][q & 3] += v; else acc[q >> 2][q & 3] += v; }
  }
  TSTAMP(3);
  u32x4 own[4];
#pragma unroll
  for (int mm = 0; mm < 2; ++mm)
#pragma unroll
    for (int np = 0; np < 2; ++np) {
      f32x4 v0 = grp ? acc[2 + mm][2 * np] : acc[mm][2 * np], v1 = grp ? acc[2 + mm][2 * np + 1] : acc[mm][2 * np + 1];
      if (role) {
#pragma unroll
        for (int e = 0; e < 4; ++e) { v0[e] = silu_f(rbf(v0[e])); v1[e] = silu_f(rbf(v1[e])); }
      }
      const unsigned a0 = pk2bf(v0[0], v0[1]), a1 = pk2bf(v0[2], v0[3]), b0 = pk2bf(v1[0], v1[1]), b1 = pk2bf(v1[2], v1[3]);
      const auto r0 = __builtin_amdgcn_permlane16_swap(a0, b0, false, false);
      const auto r1 = __builtin_amdgcn_permlane16_swap(a1, b1, false, false);
      own[mm * 2 + np] = (u32x4){r0[0], r1[0], r0[1], r1[1]};
    }
  TSTAMP(4);
  if (ticket == 0u) {
#pragma unroll
    for (int q = 0; q < 4; ++q) __builtin_amdgcn_raw_buffer_store_b128(own[q], rs, (eoff + (q >> 1) * 16 * DM + (q & 1) * 32) * 2, 0, 16);
    asm volatile("s_waitcnt vmcnt(0)" ::: "memory");
    TSTAMP(5);
    if (lane == 0) __hip_atomic_store(flag, 1u, __ATOMIC_RELAXED, __HIP_MEMORY_SCOPE_AGENT);
    TSTAMP(6);
  } else {
#pragma unroll
    for (int q = 0; q < 4; ++q) {
      u32x4 w;
#pragma unroll
      for (int e = 0; e < 4; ++e) {
        const float p0 = rbf(__uint_as_float(own[q][e] << 16) * __uint_as_float(oth[q][e] << 16)), p1 = rbf(__uint_as_float(own[q][e] & 0xffff0000u) * __uint_as_float(oth[q][e] & 0xffff0000u));
        w[e] = pk2bf(__uint_as_float(xv[q][e] << 16) + p0, __uint_as_float(xv[q][e] & 0xffff0000u) + p1);
      }
      *reinterpret_cast<u32x4*>(out + eoff + (q >> 1) * 16 * DM + (q & 1) * 32) = w;
    }
    TSTAMP(5); TSTAMP(6);
  }
  TSTAMP(7);
  TSTAMP_FLUSH; CSTAMP_FLUSH(3);
#undef STAGE_A3
#undef STAGE_W3
#undef MMA3
#undef PHASE
  KSTAMP(3, 1);
}

namespace att {
constexpr int D = 128, NW = 8, QBLK = 32, KVBLK = 64;
constexpr float SCALE = 0.088388347648318440f;
constexpr float THR = 8.f;
constexpr int LDQ = NQ, LDK = DH, LDO = NQ;
constexpr size_t SHM_V = KVBLK * D * 2, SHM_K = KVBLK * D * 2, SHM_ATTN = 2 * SHM_V + 2 * SHM_K + NW * 64 * 4;
typedef short s16x4 __attribute__((ext_vector_type(4)));
#define KSWZ(row, colB) ((row) * 256 + ((colB) ^ (((row) & 7) << 4)))
#define SBAR() __builtin_amdgcn_sched_barrier(0)
__device__ __forceinline__ int crow(int r, int hi) { return (r & 3) + 8 * (r >> 2) + 4 * hi; }
__device__ __forceinline__ unsigned cvtpk(float lo, float hi) { return pk2bf(lo, hi); }

__device__ __forceinline__ void partialSM(f32x16& p0, f32x16& p1, float& m_reg, float& mn, float& alpha) {
  constexpr float C = SCALE * 1.4426950408889634f;
  float pmax = p0[0];
#pragma unroll
  for (int r = 1; r < 16; ++r) pmax = fmaxf(pmax, p0[r]);
#pragma unroll
  for (int r = 0; r < 16; ++r) pmax = fmaxf(pmax, p1[r]);
  { auto rr = __builtin_amdgcn_permlane32_swap(__float_as_uint(pmax), __float_as_uint(pmax), false, false);
    pmax = fmaxf(__uint_as_float(rr[0]), __uint_as_float(rr[1])); }
  if (__builtin_expect(__all(pmax - m_reg <= THR / SCALE), 1)) { mn = m_reg; alpha = 1.f; }
  else { mn = fmaxf(m_reg, pmax); alpha = __builtin_amdgcn_exp2f((m_reg - mn) * C); m_reg = mn; }
  const float mnC = -mn * C;
#pragma unroll
  for (int r = 0; r < 16; ++r) p0[r] = fmaf(p0[r], C, mnC);
#pragma unroll
  for (int r = 0; r < 16; ++r) p1[r] = fmaf(p1[r], C, mnC);
#pragma unroll
  for (int r = 0; r < 16; ++r) p0[r] = __builtin_amdgcn_exp2f(p0[r]);
}
__device__ __forceinline__ void finishSM(f32x16& p0, f32x16& p1, float alpha, float& l_reg, bf16x8& pa0, bf16x8& pa1, bf16x8& pa2, bf16x8& pa3) {
#pragma unroll
  for (int r = 0; r < 16; ++r) p1[r] = __builtin_amdgcn_exp2f(p1[r]);
  float ps = 0;
#pragma unroll
  for (int r = 0; r < 16; ++r) ps += p0[r];
#pragma unroll
  for (int r = 0; r < 16; ++r) ps += p1[r];
  { auto rr = __builtin_amdgcn_permlane32_swap(__float_as_uint(ps), __float_as_uint(ps), false, false);
    ps = __uint_as_float(rr[0]) + __uint_as_float(rr[1]); }
  l_reg = l_reg * alpha + ps;
#define PK4(P, BASE, OUT) do { unsigned a0 = cvtpk(P[BASE + 0], P[BASE + 1]), a1 = cvtpk(P[BASE + 2], P[BASE + 3]);   \
    unsigned b0 = cvtpk(P[BASE + 4], P[BASE + 5]), b1 = cvtpk(P[BASE + 6], P[BASE + 7]);                              \
    auto r0 = __builtin_amdgcn_permlane32_swap(a0, b0, false, false); auto r1 = __builtin_amdgcn_permlane32_swap(a1, b1, false, false); \
    u32x4 w = {r0[0], r1[0], r0[1], r1[1]}; OUT = *reinterpret_cast<bf16x8*>(&w); } while (0)
  PK4(p0, 0, pa0); PK4(p0, 8, pa1); PK4(p1, 0, pa2); PK4(p1, 8, pa3);
#undef PK4
}
__device__ __forceinline__ void qkt(f32x16& p0, f32x16& p1, const char* Ks, const bf16x8* qr, int r32, int hi) {
  p0 = f32x16{}; p1 = f32x16{};
#pragma unroll
  for (int d0 = 0; d0 < 8; ++d0) { const int cb = (d0 * 16 + hi * 8) * 2;
    const bf16x8 b0 = *reinterpret_cast<const bf16x8*>(Ks + KSWZ(r32, cb));
    const bf16x8 b1 = *reinterpret_cast<const bf16x8*>(Ks + KSWZ(32 + r32, cb));
    p0 = __builtin_amdgcn_mfma_f32_32x32x16_bf16(b0, qr[d0], p0, 0, 0, 0);
    p1 = __builtin_amdgcn_mfma_f32_32x32x16_bf16(b1, qr[d0], p1, 0, 0, 0); }
}
__device__ __forceinline__ int v_st(int k, int c) { const int kk = (k & ~0xC) | ((k & 4) << 1) | ((k & 8) >> 1); return ((kk >> 3) * 4 + (c >> 5)) * 512 + ((kk & 7) * 32 + (c & 31)) * 2; }
__device__ __forceinline__ int v_rd_base(int lane) { return ((lane & 3) << 3) | (((lane >> 2) & 3) << 6) | (((lane >> 4) & 1) << 5) | (((lane >> 5) & 1) << 8); }
constexpr int v_rd_off(int d0, int ks, int half) { return d0 * 512 + ks * 4096 + half * 2048; }
template <int OFF> __device__ __forceinline__ s16x4 tr_read(int vb) {
  s16x4 r; asm volatile("ds_read_b64_tr_b16 %0, %1 offset:%2" : "=&v"(r) : "v"(vb), "i"(OFF) : "memory"); return r;
}
template <int D0> __device__ __forceinline__ void pv_one(f32x16& od, int vb, bf16x8 pa0, bf16x8 pa1, bf16x8 pa2, bf16x8 pa3) {
  const s16x4 l0 = tr_read<v_rd_off(D0, 0, 0)>(vb), h0 = tr_read<v_rd_off(D0, 0, 1)>(vb), l1 = tr_read<v_rd_off(D0, 1, 0)>(vb), h1 = tr_read<v_rd_off(D0, 1, 1)>(vb);
  const s16x4 l2 = tr_read<v_rd_off(D0, 2, 0)>(vb), h2 = tr_read<v_rd_off(D0, 2, 1)>(vb), l3 = tr_read<v_rd_off(D0, 3, 0)>(vb), h3 = tr_read<v_rd_off(D0, 3, 1)>(vb);
  asm volatile("s_waitcnt lgkmcnt(0)" ::: "memory"); SBAR();
#define PK(L, H) (bf16x8){L[0], L[1], L[2], L[3], H[0], H[1], H[2], H[3]}
  od = __builtin_amdgcn_mfma_f32_32x32x16_bf16(pa0, PK(l0, h0), od, 0, 0, 0);
  od = __builtin_amdgcn_mfma_f32_32x32x16_bf16(pa1, PK(l1, h1), od, 0, 0, 0);
  od = __builtin_amdgcn_mfma_f32_32x32x16_bf16(pa2, PK(l2, h2), od, 0, 0, 0);
  od = __builtin_amdgcn_mfma_f32_32x32x16_bf16(pa3, PK(l3, h3), od, 0, 0, 0);
#undef PK
}
__device__ __forceinline__ void pv_d0(f32x16* o, int vb, bf16x8 pa0, bf16x8 pa1, bf16x8 pa2, bf16x8 pa3) {
  pv_one<0>(o[0], vb, pa0, pa1, pa2, pa3); pv_one<1>(o[1], vb, pa0, pa1, pa2, pa3); pv_one<2>(o[2], vb, pa0, pa1, pa2, pa3); pv_one<3>(o[3], vb, pa0, pa1, pa2, pa3);
}

__device__ __forceinline__ void attn_body(const u16* __restrict__ Qb, const u16* __restrict__ Kh, const u16* __restrict__ Vh, u16* __restrict__ Ob, int seq, char* lds) {
  const int tid = threadIdx.x, wid = tid >> 6, lane = tid & 63, r32 = lane & 31, hi = lane >> 5;
  char* V_lds = lds; char* K_lds = lds + 2 * SHM_V;
  float* ws = (float*)(lds + 2 * SHM_V + 2 * SHM_K) + wid * 64; float* li_l = ws; float* al_l = ws + 32;
  float m_reg = -1e30f, l_reg = 0; f32x16 o[4] = {}; bf16x8 qr[8];
  const u16* Qw = Qb + (size_t)(wid * QBLK + r32) * LDQ + hi * 8;
#pragma unroll
  for (int d0 = 0; d0 < 8; ++d0) qr[d0] = *reinterpret_cast<const bf16x8*>(Qw + d0 * 16);
  const int sr = tid >> 4, sc = (tid & 15) * 8, vst0 = v_st(sr, sc), vst1 = v_st(32 + sr, sc);
  const int vb0 = (int)(uintptr_t)V_lds + v_rd_base(lane);
  struct { bf16x8 vs0, vs1, ks0, ks1; } sr_[2];
#define SLOAD(i, k0) do { sr_[i].vs0 = *reinterpret_cast<const bf16x8*>(&Vh[(size_t)((k0) + sr) * LDK + sc]); sr_[i].vs1 = *reinterpret_cast<const bf16x8*>(&Vh[(size_t)((k0) + 32 + sr) * LDK + sc]); \
    sr_[i].ks0 = *reinterpret_cast<const bf16x8*>(&Kh[(size_t)((k0) + sr) * LDK + sc]); sr_[i].ks1 = *reinterpret_cast<const bf16x8*>(&Kh[(size_t)((k0) + 32 + sr) * LDK + sc]); } while (0)
#define SWRITE(b, i) do { *(bf16x8*)(V_lds + (b) * SHM_V + vst0) = sr_[i].vs0;          \
    *(bf16x8*)(V_lds + (b) * SHM_V + vst1) = sr_[i].vs1; int kc = sc * 2;               \
    *(bf16x8*)(K_lds + (b) * SHM_K + KSWZ(sr, kc)) = sr_[i].ks0;                       \
    *(bf16x8*)(K_lds + (b) * SHM_K + KSWZ(32 + sr, kc)) = sr_[i].ks1; } while (0)
#define SWAIT() asm volatile("s_waitcnt vmcnt(4)" ::: "memory")
#define RESC(a) do { if (__any((a) < 1.f)) { if (hi == 0) al_l[r32] = (a); asm volatile("s_waitcnt lgkmcnt(0)" ::: "memory"); \
    _Pragma("unroll") for (int d = 0; d < 4; ++d) _Pragma("unroll") for (int r = 0; r < 16; ++r) o[d][r] *= al_l[crow(r, hi)]; } } while (0)
  f32x16 pA0, pA1, pB0, pB1; float mnA, mnB, alA, alB; bf16x8 pa0, pa1, pa2, pa3; const int NT = seq / KVBLK;
  constexpr int SE = 0, SO = 1;
  SLOAD(SE, 0); asm volatile("s_waitcnt vmcnt(0)" ::: "memory"); SWRITE(0, SE); __syncthreads();
  qkt(pA0, pA1, K_lds, qr, r32, hi); partialSM(pA0, pA1, m_reg, mnA, alA);
  SLOAD(SO, KVBLK); if (2 < NT) SLOAD(SE, 2 * KVBLK);
  SWAIT(); SWRITE(1, SO); __syncthreads();
  for (int j = 1; j + 1 < NT; j += 2) {
    SBAR(); qkt(pB0, pB1, K_lds + SHM_K, qr, r32, hi);
    finishSM(pA0, pA1, alA, l_reg, pa0, pa1, pa2, pa3); SBAR();
    SLOAD(SO, (j + 2) * KVBLK); SBAR();
    pv_d0(o, vb0, pa0, pa1, pa2, pa3); partialSM(pB0, pB1, m_reg, mnB, alB);
    __syncthreads(); SWAIT(); SWRITE(0, SE);
    RESC(alB); __syncthreads();
    SBAR(); qkt(pA0, pA1, K_lds, qr, r32, hi);
    finishSM(pB0, pB1, alB, l_reg, pa0, pa1, pa2, pa3); SBAR();
    if (j + 3 < NT) SLOAD(SE, (j + 3) * KVBLK); SBAR();
    pv_d0(o, vb0 + (int)SHM_V, pa0, pa1, pa2, pa3); partialSM(pA0, pA1, m_reg, mnA, alA);
    __syncthreads(); SWAIT(); SWRITE(1, SO);
    RESC(alA); __syncthreads();
  }
  SBAR(); qkt(pB0, pB1, K_lds + SHM_K, qr, r32, hi);
  finishSM(pA0, pA1, alA, l_reg, pa0, pa1, pa2, pa3); SBAR();
  pv_d0(o, vb0, pa0, pa1, pa2, pa3); partialSM(pB0, pB1, m_reg, mnB, alB);
  __syncthreads(); RESC(alB);
  finishSM(pB0, pB1, alB, l_reg, pa0, pa1, pa2, pa3); SBAR();
  pv_d0(o, vb0 + (int)SHM_V, pa0, pa1, pa2, pa3);
  if (hi == 0) li_l[r32] = l_reg; asm volatile("s_waitcnt lgkmcnt(0)" ::: "memory");
  float rli[16];
#pragma unroll
  for (int r = 0; r < 16; ++r) rli[r] = __builtin_amdgcn_rcpf(li_l[crow(r, hi)]);
  u16* Ow = Ob + (size_t)(wid * QBLK) * LDO;
#pragma unroll
  for (int r = 0; r < 16; ++r) { const int orow = crow(r, hi);
#pragma unroll
    for (int d0 = 0; d0 < 4; ++d0) Ow[(size_t)orow * LDO + d0 * 32 + r32] = (u16)(pk2bf(o[d0][r] * rli[r], 0.f) & 0xffff); }
#undef SLOAD
#undef SWRITE
#undef SWAIT
#undef RESC
}
}

__global__ __launch_bounds__(512, 2) void attn_kernel(const u16* __restrict__ Q, const u16* __restrict__ Kc, const u16* __restrict__ Vc, u16* __restrict__ O, unsigned long long* stamp_buf) {
  extern __shared__ __attribute__((aligned(16))) char shm_[];
  KSTAMP(2, 0);
  const int bid = blockIdx.x;
  const int h = bid >> 3, rb = bid & 7, g = h >> 3;
  const size_t q0 = (size_t)rb * 256 * NQ + h * DH, k0 = ((size_t)g * SK + (SK - WIN)) * DH;
  att::attn_body(Q + q0, Kc + k0, Vc + k0, O + q0, WIN, shm_);
  KSTAMP(2, 1);
}

extern "C" void kernel_launch(void* const* d_in, const int* in_sizes, int n_in, void* d_out, int out_size, void* d_ws, size_t ws_size, hipStream_t stream) {
  const u16* x = (const u16*)d_in[0]; const u16* kc = (const u16*)d_in[1]; const u16* vc = (const u16*)d_in[2];
  const u16* wq = (const u16*)d_in[3]; const u16* wo = (const u16*)d_in[4]; const u16* w1 = (const u16*)d_in[6]; const u16* w2 = (const u16*)d_in[7];
  char* ws = (char*)d_ws;
  u16* xn = (u16*)(ws + WS_XN); u16* Q = (u16*)(ws + WS_Q); u16* H = (u16*)(ws + WS_H); u16* O = (u16*)(ws + WS_O);
  hipFuncSetAttribute((const void*)gemm1_kernel, hipFuncAttributeMaxDynamicSharedMemorySize, G_SHM);
  hipFuncSetAttribute((const void*)gemm3_kernel, hipFuncAttributeMaxDynamicSharedMemorySize, G3_SHM);
  hipFuncSetAttribute((const void*)attn_kernel, hipFuncAttributeMaxDynamicSharedMemorySize, (int)att::SHM_ATTN);
  unsigned long long* sb = (unsigned long long*)(ws + WS_STAMP);
  unsigned* sync = (unsigned*)(ws + WS_SYNC);
  ln_kernel<<<SEQ / 4, 256, 0, stream>>>(x, xn, sync, sb);
  gemm1_kernel<<<240, 512, G_SHM, stream>>>(xn, wq, w1, Q, H, sb);
  attn_kernel<<<256, 512, att::SHM_ATTN, stream>>>(Q, kc, vc, O, sb);
  gemm3_kernel<<<256, 512, G3_SHM, stream>>>(O, H, wo, w2, x, (u16*)d_out, (u16*)(ws + WS_XCH), sync, sb);
}
```

```cpp
#include <hip/hip_runtime.h>
#include <hip/hip_bf16.h>
#include <stdint.h>

typedef unsigned short u16;
typedef short bf16x8 __attribute__((ext_vector_type(8)));
typedef short bf16x4 __attribute__((ext_vector_type(4)));
typedef float f32x4 __attribute__((ext_vector_type(4)));
typedef float f32x16 __attribute__((ext_vector_type(16)));
typedef unsigned u32x4 __attribute__((ext_vector_type(4)));
typedef unsigned u32x2 __attribute__((ext_vector_type(2)));
#define LAS __attribute__((address_space(3)))

constexpr int SEQ = 2048, DM = 1024, NQ = 4096, DFF = 3584, NH = 32, NKV = 4, DH = 128, SK = 2048, WIN = 512;
constexpr int LDO_ = NQ;

constexpr size_t WS_XN   = 0;
constexpr size_t WS_H    = WS_XN + (size_t)SEQ * DM * 2;
constexpr size_t WS_O    = WS_H + (size_t)SEQ * DFF * 2;
constexpr size_t WS_XCH  = WS_O + (size_t)SEQ * LDO_ * 2;
constexpr size_t WS_SYNC = WS_XCH + (size_t)SEQ * DM * 2;

__device__ __forceinline__ float bf2f(u16 h) { return __uint_as_float((unsigned)h << 16); }
__device__ __forceinline__ unsigned pk2bf(float lo, float hi) {
  unsigned r; asm volatile("v_cvt_pk_bf16_f32 %0, %1, %2" : "=v"(r) : "v"(lo), "v"(hi)); return r;
}
__device__ __forceinline__ float rbf(float f) { return __uint_as_float(pk2bf(f, f) << 16); }
__device__ __forceinline__ float silu_f(float a) {
  return a * __builtin_amdgcn_rcpf(1.f + __builtin_amdgcn_exp2f(a * -1.4426950408889634f));
}

__device__ __forceinline__ float wave_sum(float x) {
  x += __int_as_float(__builtin_amdgcn_update_dpp(0, __float_as_int(x), 0xB1, 0xF, 0xF, true));
  x += __int_as_float(__builtin_amdgcn_update_dpp(0, __float_as_int(x), 0x4E, 0xF, 0xF, true));
  x += __int_as_float(__builtin_amdgcn_update_dpp(0, __float_as_int(x), 0x141, 0xF, 0xF, true));
  x += __int_as_float(__builtin_amdgcn_update_dpp(0, __float_as_int(x), 0x140, 0xF, 0xF, true));
  const int xi = __float_as_int(x);
  return (__int_as_float(__builtin_amdgcn_readlane(xi, 0)) + __int_as_float(__builtin_amdgcn_readlane(xi, 16))) +
         (__int_as_float(__builtin_amdgcn_readlane(xi, 32)) + __int_as_float(__builtin_amdgcn_readlane(xi, 48)));
}

__global__ __launch_bounds__(256) void ln_kernel(const u16* __restrict__ x, u16* __restrict__ xn, unsigned* __restrict__ sync) {
  if (blockIdx.x < 32) sync[blockIdx.x * 256 + threadIdx.x] = 0u;
  const int lane = threadIdx.x & 63, row = blockIdx.x * 4 + (threadIdx.x >> 6);
  const u16* xr = x + (size_t)row * DM;
  const bf16x8 a = *reinterpret_cast<const bf16x8*>(xr + lane * 8), b = *reinterpret_cast<const bf16x8*>(xr + 512 + lane * 8);
  float v[16];
#pragma unroll
  for (int i = 0; i < 8; ++i) { v[i] = bf2f((u16)a[i]); v[8 + i] = bf2f((u16)b[i]); }
  float s = 0.f;
#pragma unroll
  for (int i = 0; i < 16; ++i) s += v[i];
  const float mean = wave_sum(s) * (1.0f / DM);
  float q = 0.f;
#pragma unroll
  for (int i = 0; i < 16; ++i) { const float d = v[i] - mean; q += d * d; }
  const float mb = rbf(mean), vb = rbf(wave_sum(q) * (1.0f / DM));
  const float den = rbf(sqrtf(rbf(vb + 1e-5f)));
  u32x4 w0, w1;
#pragma unroll
  for (int i = 0; i < 4; ++i) {
    w0[i] = pk2bf(rbf(v[2 * i] - mb) / den, rbf(v[2 * i + 1] - mb) / den);
    w1[i] = pk2bf(rbf(v[8 + 2 * i] - mb) / den, rbf(v[8 + 2 * i + 1] - mb) / den);
  }
  u16* o = xn + (size_t)row * DM;
  *reinterpret_cast<u32x4*>(o + lane * 8) = w0;
  *reinterpret_cast<u32x4*>(o + 512 + lane * 8) = w1;
}

namespace att {
constexpr int D = 128, NW = 8, QBLK = 32, KVBLK = 64;
constexpr float SCALE = 0.088388347648318440f;
constexpr float THR = 8.f;
constexpr int LDQ = NQ, LDK = DH, LDO = LDO_;
constexpr size_t SHM_V = KVBLK * D * 2, SHM_K = KVBLK * D * 2, SHM_ATTN = 2 * SHM_V + 2 * SHM_K + NW * 64 * 4;
typedef short s16x4 __attribute__((ext_vector_type(4)));
#define KSWZ(row, colB) ((row) * 256 + ((colB) ^ (((row) & 7) << 4)))
#define SBAR() __builtin_amdgcn_sched_barrier(0)
__device__ __forceinline__ int crow(int r, int hi) { return (r & 3) + 8 * (r >> 2) + 4 * hi; }
__device__ __forceinline__ unsigned cvtpk(float lo, float hi) { return pk2bf(lo, hi); }

__device__ __forceinline__ void partialSM(f32x16& p0, f32x16& p1, float& m_reg, float& mn, float& alpha) {
  constexpr float C = SCALE * 1.4426950408889634f;
  float pmax = p0[0];
#pragma unroll
  for (int r = 1; r < 16; ++r) pmax = fmaxf(pmax, p0[r]);
#pragma unroll
  for (int r = 0; r < 16; ++r) pmax = fmaxf(pmax, p1[r]);
  { auto rr = __builtin_amdgcn_permlane32_swap(__float_as_uint(pmax), __float_as_uint(pmax), false, false);
    pmax = fmaxf(__uint_as_float(rr[0]), __uint_as_float(rr[1])); }
  if (__builtin_expect(__all(pmax - m_reg <= THR / SCALE), 1)) { mn = m_reg; alpha = 1.f; }
  else { mn = fmaxf(m_reg, pmax); alpha = __builtin_amdgcn_exp2f((m_reg - mn) * C); m_reg = mn; }
  const float mnC = -mn * C;
#pragma unroll
  for (int r = 0; r < 16; ++r) p0[r] = fmaf(p0[r], C, mnC);
#pragma unroll
  for (int r = 0; r < 16; ++r) p1[r] = fmaf(p1[r], C, mnC);
#pragma unroll
  for (int r = 0; r < 16; ++r) p0[r] = __builtin_amdgcn_exp2f(p0[r]);
}
__device__ __forceinline__ void finishSM(f32x16& p0, f32x16& p1, float alpha, float& l_reg, bf16x8& pa0, bf16x8& pa1, bf16x8& pa2, bf16x8& pa3) {
#pragma unroll
  for (int r = 0; r < 16; ++r) p1[r] = __builtin_amdgcn_exp2f(p1[r]);
  float ps = 0;
#pragma unroll
  for (int r = 0; r < 16; ++r) ps += p0[r];
#pragma unroll
  for (int r = 0; r < 16; ++r) ps += p1[r];
  { auto rr = __builtin_amdgcn_permlane32_swap(__float_as_uint(ps), __float_as_uint(ps), false, false);
    ps = __uint_as_float(rr[0]) + __uint_as_float(rr[1]); }
  l_reg = l_reg * alpha + ps;
#define PK4(P, BASE, OUT) do { unsigned a0 = cvtpk(P[BASE + 0], P[BASE + 1]), a1 = cvtpk(P[BASE + 2], P[BASE + 3]);   \
    unsigned b0 = cvtpk(P[BASE + 4], P[BASE + 5]), b1 = cvtpk(P[BASE + 6], P[BASE + 7]);                              \
    auto r0 = __builtin_amdgcn_permlane32_swap(a0, b0, false, false); auto r1 = __builtin_amdgcn_permlane32_swap(a1, b1, false, false); \
    u32x4 w = {r0[0], r1[0], r0[1], r1[1]}; OUT = *reinterpret_cast<bf16x8*>(&w); } while (0)
  PK4(p0, 0, pa0); PK4(p0, 8, pa1); PK4(p1, 0, pa2); PK4(p1, 8, pa3);
#undef PK4
}
__device__ __forceinline__ void qkt(f32x16& p0, f32x16& p1, const char* Ks, const bf16x8* qr, int r32, int hi) {
  p0 = f32x16{}; p1 = f32x16{};
#pragma unroll
  for (int d0 = 0; d0 < 8; ++d0) { const int cb = (d0 * 16 + hi * 8) * 2;
    const bf16x8 b0 = *reinterpret_cast<const bf16x8*>(Ks + KSWZ(r32, cb));
    const bf16x8 b1 = *reinterpret_cast<const bf16x8*>(Ks + KSWZ(32 + r32, cb));
    p0 = __builtin_amdgcn_mfma_f32_32x32x16_bf16(b0, qr[d0], p0, 0, 0, 0);
    p1 = __builtin_amdgcn_mfma_f32_32x32x16_bf16(b1, qr[d0], p1, 0, 0, 0); }
}
__device__ __forceinline__ int v_st(int k, int c) { const int kk = (k & ~0xC) | ((k & 4) << 1) | ((k & 8) >> 1); return ((kk >> 3) * 4 + (c >> 5)) * 512 + ((kk & 7) * 32 + (c & 31)) * 2; }
__device__ __forceinline__ int v_rd_base(int lane) { return ((lane & 3) << 3) | (((lane >> 2) & 3) << 6) | (((lane >> 4) & 1) << 5) | (((lane >> 5) & 1) << 8); }
constexpr int v_rd_off(int d0, int ks, int half) { return d0 * 512 + ks * 4096 + half * 2048; }
template <int OFF> __device__ __forceinline__ s16x4 tr_read(int vb) {
  s16x4 r; asm volatile("ds_read_b64_tr_b16 %0, %1 offset:%2" : "=&v"(r) : "v"(vb), "i"(OFF) : "memory"); return r;
}
template <int D0> __device__ __forceinline__ void pv_one(f32x16& od, int vb, bf16x8 pa0, bf16x8 pa1, bf16x8 pa2, bf16x8 pa3) {
  const s16x4 l0 = tr_read<v_rd_off(D0, 0, 0)>(vb), h0 = tr_read<v_rd_off(D0, 0, 1)>(vb), l1 = tr_read<v_rd_off(D0, 1, 0)>(vb), h1 = tr_read<v_rd_off(D0, 1, 1)>(vb);
  const s16x4 l2 = tr_read<v_rd_off(D0, 2, 0)>(vb), h2 = tr_read<v_rd_off(D0, 2, 1)>(vb), l3 = tr_read<v_rd_off(D0, 3, 0)>(vb), h3 = tr_read<v_rd_off(D0, 3, 1)>(vb);
  asm volatile("s_waitcnt lgkmcnt(0)" ::: "memory"); SBAR();
#define PK(L, H) (bf16x8){L[0], L[1], L[2], L[3], H[0], H[1], H[2], H[3]}
  od = __builtin_amdgcn_mfma_f32_32x32x16_bf16(pa0, PK(l0, h0), od, 0, 0, 0);
  od = __builtin_amdgcn_mfma_f32_32x32x16_bf16(pa1, PK(l1, h1), od, 0, 0, 0);
  od = __builtin_amdgcn_mfma_f32_32x32x16_bf16(pa2, PK(l2, h2), od, 0, 0, 0);
  od = __builtin_amdgcn_mfma_f32_32x32x16_bf16(pa3, PK(l3, h3), od, 0, 0, 0);
#undef PK
}
__device__ __forceinline__ void pv_d0(f32x16* o, int vb, bf16x8 pa0, bf16x8 pa1, bf16x8 pa2, bf16x8 pa3) {
  pv_one<0>(o[0], vb, pa0, pa1, pa2, pa3); pv_one<1>(o[1], vb, pa0, pa1, pa2, pa3); pv_one<2>(o[2], vb, pa0, pa1, pa2, pa3); pv_one<3>(o[3], vb, pa0, pa1, pa2, pa3);
}

struct KVStage { bf16x8 vs0, vs1, ks0, ks1; };
__device__ __forceinline__ void kv_load(KVStage& st, const u16* __restrict__ Kh, const u16* __restrict__ Vh, int k0, int tid) {
  const int sr = tid >> 4, sc = (tid & 15) * 8;
  st.vs0 = *reinterpret_cast<const bf16x8*>(&Vh[(size_t)(k0 + sr) * LDK + sc]); st.vs1 = *reinterpret_cast<const bf16x8*>(&Vh[(size_t)(k0 + 32 + sr) * LDK + sc]);
  st.ks0 = *reinterpret_cast<const bf16x8*>(&Kh[(size_t)(k0 + sr) * LDK + sc]); st.ks1 = *reinterpret_cast<const bf16x8*>(&Kh[(size_t)(k0 + 32 + sr) * LDK + sc]);
}
__device__ __forceinline__ void attn_body(const char* Qlds, const u16* __restrict__ Kh, const u16* __restrict__ Vh, u16* __restrict__ Ob, int seq, char* lds,
                                          KVStage (&sr_)[2]  ,
                                          const char* pf_base, unsigned pf_lds  ) {
  int tid = threadIdx.x; asm volatile("" : "+v"(tid));
  const int wid = __builtin_amdgcn_readfirstlane(tid >> 6), lane = tid & 63, r32 = lane & 31, hi = lane >> 5;
  char* V_lds = lds; char* K_lds = lds + 2 * SHM_V;
  float* ws = (float*)(lds + 2 * SHM_V + 2 * SHM_K) + wid * 64; float* li_l = ws; float* al_l = ws + 32;
  float m_reg = -1e30f, l_reg = 0; f32x16 o[4] = {}; bf16x8 qr[8];
#pragma unroll
  for (int d0 = 0; d0 < 8; ++d0) qr[d0] = *reinterpret_cast<const bf16x8*>(Qlds + KSWZ(wid * QBLK + r32, (d0 * 16 + hi * 8) * 2));
  const int sr = tid >> 4, sc = (tid & 15) * 8, vst0 = v_st(sr, sc), vst1 = v_st(32 + sr, sc);
  const int vb0 = (int)(uintptr_t)V_lds + v_rd_base(lane);
#define SLOAD(i, k0) do { sr_[i].vs0 = *reinterpret_cast<const bf16x8*>(&Vh[(size_t)((k0) + sr) * LDK + sc]); sr_[i].vs1 = *reinterpret_cast<const bf16x8*>(&Vh[(size_t)((k0) + 32 + sr) * LDK + sc]); \
    sr_[i].ks0 = *reinterpret_cast<const bf16x8*>(&Kh[(size_t)((k0) + sr) * LDK + sc]); sr_[i].ks1 = *reinterpret_cast<const bf16x8*>(&Kh[(size_t)((k0) + 32 + sr) * LDK + sc]); } while (0)
#define SWRITE(b, i) do { *(bf16x8*)(V_lds + (b) * SHM_V + vst0) = sr_[i].vs0;          \
    *(bf16x8*)(V_lds + (b) * SHM_V + vst1) = sr_[i].vs1; int kc = sc * 2;               \
    *(bf16x8*)(K_lds + (b) * SHM_K + KSWZ(sr, kc)) = sr_[i].ks0;                       \
    *(bf16x8*)(K_lds + (b) * SHM_K + KSWZ(32 + sr, kc)) = sr_[i].ks1; } while (0)
#define SWAIT() asm volatile("s_waitcnt vmcnt(4)" ::: "memory")
#define PF_TOUCH(slot) do { if (pf_slot == (slot)) { unsigned keep_, vo_ = (unsigned)vst0; asm volatile("" : "+v"(vo_)); vo_ <<= 3; \
    asm volatile("s_mov_b32 %0, m0\n\ts_mov_b32 m0, %2\n\ts_nop 0\n\tglobal_load_lds_dword %1, %3\n\ts_mov_b32 m0, %0" : "=&s"(keep_) : "v"(vo_), "s"(pf_lds), "s"(pf_base) : "memory"); } } while (0)
#define RESC(a) do { if (__any((a) < 1.f)) { if (hi == 0) al_l[r32] = (a); asm volatile("s_waitcnt lgkmcnt(0)" ::: "memory"); \
    _Pragma("unroll") for (int d = 0; d < 4; ++d) _Pragma("unroll") for (int r = 0; r < 16; ++r) o[d][r] *= al_l[crow(r, hi)]; } } while (0)
  f32x16 pA0, pA1, pB0, pB1; float mnA, mnB, alA, alB; bf16x8 pa0, pa1, pa2, pa3; const int NT = seq / KVBLK;
  constexpr int SE = 0, SO = 1;
  const int pf_slot = wid % 6;
  SWRITE(0, SE); __syncthreads();
  SLOAD(SE, 2 * KVBLK);
  qkt(pA0, pA1, K_lds, qr, r32, hi); partialSM(pA0, pA1, m_reg, mnA, alA);
  SWAIT(); SWRITE(1, SO); __syncthreads();
  for (int j = 1; j + 1 < NT; j += 2) {
    SBAR(); qkt(pB0, pB1, K_lds + SHM_K, qr, r32, hi);
    finishSM(pA0, pA1, alA, l_reg, pa0, pa1, pa2, pa3); SBAR();
    SLOAD(SO, (j + 2) * KVBLK); PF_TOUCH(j - 1); SBAR();
    pv_d0(o, vb0, pa0, pa1, pa2, pa3); partialSM(pB0, pB1, m_reg, mnB, alB);
    __syncthreads(); SWAIT(); SWRITE(0, SE);
    RESC(alB); __syncthreads();
    SBAR(); qkt(pA0, pA1, K_lds, qr, r32, hi);
    finishSM(pB0, pB1, alB, l_reg, pa0, pa1, pa2, pa3); SBAR();
    if (j + 3 < NT) SLOAD(SE, (j + 3) * KVBLK); PF_TOUCH(j); SBAR();
    pv_d0(o, vb0 + (int)SHM_V, pa0, pa1, pa2, pa3); partialSM(pA0, pA1, m_reg, mnA, alA);
    __syncthreads(); SWAIT(); SWRITE(1, SO);
    RESC(alA); __syncthreads();
  }
  SBAR(); qkt(pB0, pB1, K_lds + SHM_K, qr, r32, hi);
  finishSM(pA0, pA1, alA, l_reg, pa0, pa1, pa2, pa3); SBAR();
  pv_d0(o, vb0, pa0, pa1, pa2, pa3); partialSM(pB0, pB1, m_reg, mnB, alB);
  __syncthreads(); RESC(alB);
  finishSM(pB0, pB1, alB, l_reg, pa0, pa1, pa2, pa3); SBAR();
  pv_d0(o, vb0 + (int)SHM_V, pa0, pa1, pa2, pa3);
  if (hi == 0) li_l[r32] = l_reg; asm volatile("s_waitcnt lgkmcnt(0)" ::: "memory");
  float rli[16];
#pragma unroll
  for (int r = 0; r < 16; ++r) rli[r] = __builtin_amdgcn_rcpf(li_l[crow(r, hi)]);
  u16* Ow = Ob + (size_t)(wid * QBLK) * LDO;
#pragma unroll
  for (int r = 0; r < 16; ++r) { const int orow = crow(r, hi);
#pragma unroll
    for (int d0 = 0; d0 < 4; ++d0) Ow[(size_t)orow * LDO + d0 * 32 + r32] = (u16)(pk2bf(o[d0][r] * rli[r], 0.f) & 0xffff); }
#undef SLOAD
#undef SWRITE
#undef SWAIT
#undef RESC
#undef PF_TOUCH
}
}

constexpr int G_HTB = 16384, G_SHM = 8 * G_HTB;
#define WAIT_V(n) asm volatile("s_waitcnt vmcnt(" #n ")" ::: "memory")
#define WAIT_L(n) asm volatile("s_waitcnt lgkmcnt(" #n ")" ::: "memory")
#define WL0 do { asm volatile("s_waitcnt lgkmcnt(0)" ::: "memory"); __builtin_amdgcn_sched_barrier(0); } while (0)
#define BAR __builtin_amdgcn_s_barrier()
#define SCHED __builtin_amdgcn_sched_barrier(0)

__device__ __forceinline__ void glds16(const void* g, LAS char* l) {
  __builtin_amdgcn_global_load_lds((const unsigned*)g, (LAS unsigned*)l, 16, 0, 0);
}
__device__ __forceinline__ bf16x8 ld_a(const LAS char* p) { return *reinterpret_cast<const LAS bf16x8*>(p); }
template <int OFF> __device__ __forceinline__ bf16x8 ld_w(int addr) {
  bf16x4 lo, hi;
  asm volatile("ds_read_b64_tr_b16 %0, %1 offset:%2" : "=&v"(lo) : "v"(addr), "i"(OFF) : "memory");
  asm volatile("ds_read_b64_tr_b16 %0, %1 offset:%2" : "=&v"(hi) : "v"(addr), "i"(OFF + 1024) : "memory");
  return (bf16x8){lo[0], lo[1], lo[2], lo[3], hi[0], hi[1], hi[2], hi[3]};
}

constexpr int F_PF = 65536 + (int)att::SHM_ATTN;
constexpr int F_SHM = F_PF + 2048;
static_assert(F_SHM >= G_SHM, "LDS");
constexpr int HS = DFF / NH;

__global__ __launch_bounds__(512, 2) void fused1_kernel(const u16* __restrict__ xn, const u16* __restrict__ wq, const u16* __restrict__ w1,
                                                       const u16* __restrict__ Kc, const u16* __restrict__ Vc, u16* __restrict__ H, u16* __restrict__ O,
                                                       const u16* __restrict__ wo, const u16* __restrict__ w2) {
  extern __shared__ __attribute__((aligned(16))) char shm_[];
  LAS char* shm = (LAS char*)shm_;
  const int bid = blockIdx.x, xcd = bid & 7, idx = bid >> 3, rb = idx & 7, hd = xcd * 4 + (idx >> 3), brow = rb * 256;
  const int tid = threadIdx.x;
  unsigned aoff[2], woff0[2], woff1[2];
  const int wid = tid >> 6, lane = tid & 63, wr = wid >> 2, wc = wid & 3, fr = lane & 15, fq = lane >> 4;
#pragma unroll
  for (int i = 0; i < 2; ++i) {
    const int r8 = lane >> 3, cp8 = lane & 7, sA = (i << 2) | (r8 >> 1);
    aoff[i] = (unsigned)((brow + (wid * 2 + i) * 8 + r8) * DM + 8 * (cp8 ^ sA));
    const int b = tid * 16 + i * 8192;
    const int k = b >> 8, cp = (b >> 4) & 15, f = (k & 3) | (((k >> 3) & 1) << 2), c = cp ^ (f << 1);
    woff0[i] = (unsigned)(k * NQ + hd * 128 + 8 * c);
    woff1[i] = (unsigned)(k * DFF + hd * HS + 8 * (c < 13 ? c : 13));
  }
#define SA(b, h) (shm + ((b) * 2 + (h)) * G_HTB)
#define SB(b, h) (shm + (4 + (b) * 2 + (h)) * G_HTB)
#define STAGE_A(P, h, kt) do { _Pragma("unroll") for (int _i = 0; _i < 2; ++_i) \
    glds16(xn + (size_t)aoff[_i] + (size_t)((h) * 128) * DM + (kt) * 64, (P) + wid * 2048 + lane * 16 + _i * 1024); } while (0)
#define STAGE_B(P, h, kt) do { _Pragma("unroll") for (int _i = 0; _i < 2; ++_i) { \
    if ((h) == 0) glds16(wq + (size_t)woff0[_i] + (size_t)((kt) * 64) * NQ, (P) + tid * 16 + _i * 8192); \
    else glds16(w1 + (size_t)woff1[_i] + (size_t)((kt) * 64) * DFF, (P) + tid * 16 + _i * 8192); } } while (0)
  const int a_s = (fr >> 1) & 7, a_row = wr * 8192 + (fr >> 3) * 1024 + (fr & 7) * 128;
  const int a_base0 = a_row + ((fq ^ a_s) << 4), a_base1 = a_row + (((4 + fq) ^ a_s) << 4);
  const int tq = (lane >> 2) & 3, tp = lane & 3, tf = tq | ((fq & 1) << 2);
  const int w_base0 = (int)(uintptr_t)shm + 4 * G_HTB + (8 * fq + tq) * 256 + ((wc * 64 + 8 * tp) ^ (tf << 5));
  const int w_base1 = (int)(uintptr_t)shm + 4 * G_HTB + (8 * fq + tq) * 256 + ((wc * 64 + 32 + 8 * tp) ^ (tf << 5));
#define LDA(dst, b, h) _Pragma("unroll") for (int m = 0; m < 4; ++m) { \
    dst[m][0] = ld_a(SA(b, h) + a_base0 + m * 2048); dst[m][1] = ld_a(SA(b, h) + a_base1 + m * 2048); }
#define LDB(dst, b, h) do { dst[0][0] = ld_w<((b) * 2 + (h)) * G_HTB>(w_base0); dst[1][0] = ld_w<((b) * 2 + (h)) * G_HTB>(w_base1); \
    dst[0][1] = ld_w<((b) * 2 + (h)) * G_HTB + 8192>(w_base0); dst[1][1] = ld_w<((b) * 2 + (h)) * G_HTB + 8192>(w_base1); } while (0)
#define MMA(ai, bj, At, Bt) do { __builtin_amdgcn_s_setprio(1); \
    _Pragma("unroll") for (int m = 0; m < 4; ++m) _Pragma("unroll") for (int n = 0; n < 2; ++n) _Pragma("unroll") for (int k = 0; k < 2; ++k) \
      acc[ai][bj][m][n] = __builtin_amdgcn_mfma_f32_16x16x32_bf16(Bt[n][k], At[m][k], acc[ai][bj][m][n], 0, 0, 0); \
    __builtin_amdgcn_s_setprio(0); } while (0)
  f32x4 acc[2][2][4][2] = {};
  bf16x8 At[4][2], B0[2][2], B1[2][2];
  constexpr int nt = DM / 64;
  STAGE_B(SB(0, 0), 0, 0); STAGE_A(SA(0, 0), 0, 0);
  STAGE_B(SB(0, 1), 1, 0); STAGE_A(SA(0, 1), 1, 0);
  unsigned tl0, tl1;
  { const unsigned hb = (unsigned)(((brow + (lane & 1) * 255) * DFF + hd * HS) * 2), ob = (unsigned)(((brow + (lane & 1) * 255) * LDO_ + hd * DH) * 2);
    asm volatile("global_load_dword %0, %1, %2" : "=v"(tl0) : "v"(hb), "s"(H) : "memory");
    asm volatile("global_load_dword %0, %1, %2" : "=v"(tl1) : "v"(ob), "s"(O) : "memory"); }
  if (wr == 1) BAR;
  WAIT_V(6); BAR;
  STAGE_B(SB(1, 0), 0, 1); STAGE_A(SA(1, 0), 0, 1); STAGE_B(SB(1, 1), 1, 1);
  WAIT_V(6); BAR;
  asm volatile("" :: "v"(tl0), "v"(tl1));
  for (int t = 0; t < nt - 2; t += 2) {
    LDB(B0, 0, 0); SCHED; LDA(At, 0, 0); STAGE_A(SA(1, 1), 1, t + 1);
    WAIT_L(8); BAR; WL0; MMA(0, 0, At, B0); BAR; SCHED;
    LDB(B1, 0, 1); STAGE_B(SB(0, 0), 0, t + 2);
    BAR; WL0; MMA(0, 1, At, B1); BAR;
    LDA(At, 0, 1); STAGE_A(SA(0, 0), 0, t + 2);
    BAR; WL0; MMA(1, 0, At, B0); BAR; SCHED;
    STAGE_B(SB(0, 1), 1, t + 2);
    WAIT_V(6); BAR; MMA(1, 1, At, B1); BAR;
    LDB(B0, 1, 0); SCHED; LDA(At, 1, 0); STAGE_A(SA(0, 1), 1, t + 2);
    WAIT_L(8); BAR; WL0; MMA(0, 0, At, B0); BAR; SCHED;
    LDB(B1, 1, 1); STAGE_B(SB(1, 0), 0, t + 3);
    BAR; WL0; MMA(0, 1, At, B1); BAR;
    LDA(At, 1, 1); STAGE_A(SA(1, 0), 0, t + 3);
    BAR; WL0; MMA(1, 0, At, B0); BAR; SCHED;
    STAGE_B(SB(1, 1), 1, t + 3);
    WAIT_V(6); BAR; MMA(1, 1, At, B1); BAR;
  }
  { LDB(B0, 0, 0); WAIT_V(0); LDA(At, 0, 0); STAGE_A(SA(1, 1), 1, nt - 1);
    BAR; WL0; MMA(0, 0, At, B0); BAR;
    LDB(B1, 0, 1); BAR; WL0; MMA(0, 1, At, B1); BAR;
    LDA(At, 0, 1); WAIT_V(4); BAR; WL0; MMA(1, 0, At, B0); MMA(1, 1, At, B1); BAR; }
  { LDB(B0, 1, 0); LDA(At, 1, 0); WAIT_V(2); BAR; WL0; MMA(0, 0, At, B0); BAR;
    LDB(B1, 1, 1); WAIT_V(0); BAR; WL0; MMA(0, 1, At, B1); BAR;
    LDA(At, 1, 1); BAR; WL0; MMA(1, 0, At, B0); MMA(1, 1, At, B1); BAR; }
  if (wr == 0) BAR;
  const size_t kv0 = ((size_t)(hd >> 3) * SK + (SK - WIN)) * DH;
  att::KVStage kvs[2];
  att::kv_load(kvs[0], Kc + kv0, Vc + kv0, 0, tid); att::kv_load(kvs[1], Kc + kv0, Vc + kv0, att::KVBLK, tid);
  const int ecol = wc * 32 + (fq & 1) * 16 + (fq >> 1) * 8, erow = wr * 64 + fr;
  u16* __restrict__ Hp = H + (size_t)(brow + erow) * DFF + hd * HS + ecol;
#pragma unroll
  for (int ai = 0; ai < 2; ++ai)
#pragma unroll
    for (int m = 0; m < 4; ++m) {
      const f32x4 v0 = acc[ai][1][m][0], v1 = acc[ai][1][m][1];
      const unsigned a0 = pk2bf(fmaxf(v0[0], 0.f), fmaxf(v0[1], 0.f)), a1 = pk2bf(fmaxf(v0[2], 0.f), fmaxf(v0[3], 0.f));
      const unsigned b0 = pk2bf(fmaxf(v1[0], 0.f), fmaxf(v1[1], 0.f)), b1 = pk2bf(fmaxf(v1[2], 0.f), fmaxf(v1[3], 0.f));
      const auto r0 = __builtin_amdgcn_permlane16_swap(a0, b0, false, false);
      const auto r1 = __builtin_amdgcn_permlane16_swap(a1, b1, false, false);
      const u32x4 w = {r0[0], r1[0], r0[1], r1[1]};
      if (ecol < HS) *reinterpret_cast<u32x4*>(Hp + (size_t)(ai * 128 + m * 16) * DFF) = w;
    }
#pragma unroll
  for (int ai = 0; ai < 2; ++ai)
#pragma unroll
    for (int m = 0; m < 4; ++m) {
      const f32x4 v0 = acc[ai][0][m][0], v1 = acc[ai][0][m][1];
      const unsigned a0 = pk2bf(v0[0], v0[1]), a1 = pk2bf(v0[2], v0[3]), b0 = pk2bf(v1[0], v1[1]), b1 = pk2bf(v1[2], v1[3]);
      const auto r0 = __builtin_amdgcn_permlane16_swap(a0, b0, false, false);
      const auto r1 = __builtin_amdgcn_permlane16_swap(a1, b1, false, false);
      const int row = ai * 128 + m * 16 + erow;
      *reinterpret_cast<LAS u32x4*>(shm + row * 256 + ((ecol * 2) ^ ((row & 7) << 4))) = (u32x4){r0[0], r1[0], r0[1], r1[1]};
    }
  WAIT_L(0); BAR; asm volatile("" ::: "memory");
#undef SA
#undef SB
#undef STAGE_A
#undef STAGE_B
#undef LDA
#undef LDB
#undef MMA
  const size_t o0 = (size_t)brow * LDO_ + hd * DH;
  const unsigned pfw = (unsigned)bid * 512u;
  const char* pfb = pfw < 65536u ? (const char*)wo + (size_t)pfw * 128 : (const char*)w2 + (size_t)(pfw - 65536u < 57344u ? pfw - 65536u : 0u) * 128;
  att::attn_body(shm_, Kc + kv0, Vc + kv0, O + o0, WIN, shm_ + 65536, kvs, pfb, (unsigned)(uintptr_t)shm + (unsigned)F_PF + (unsigned)__builtin_amdgcn_readfirstlane(wid) * 256u);
}

constexpr int G3_SHM = 163840;

__global__ __launch_bounds__(512, 2) void gemm3_kernel(const u16* __restrict__ O, const u16* __restrict__ H, const u16* __restrict__ wo, const u16* __restrict__ w2,
                                                      const u16* __restrict__ x, u16* __restrict__ out, u16* __restrict__ xch, unsigned* __restrict__ sync) {
  extern __shared__ __attribute__((aligned(16))) char shm_[];
  LAS char* shm = (LAS char*)shm_;
  const int bid = blockIdx.x, xcd = bid & 7, idx = bid >> 3;
  const int role = xcd >> 2, pm = (xcd & 1) * 8 + (idx & 7), pn = ((xcd >> 1) & 1) * 4 + (idx >> 3);
  const u16* __restrict__ A = role ? H : O; const u16* __restrict__ Wt = role ? w2 : wo;
  const int lda = role ? DFF : LDO_, nph = role ? DFF / 64 : NQ / 64, brow = pm * 128, bcol = pn * 128;
  const int tid = threadIdx.x, wid = __builtin_amdgcn_readfirstlane(tid >> 6), lane = tid & 63, grp = wid >> 2, w4 = wid & 3, wr = w4 >> 1, wc = w4 & 1, fr = lane & 15, fq = lane >> 4;
  const int tq = (lane >> 2) & 3, tp = lane & 3, tf = tq | ((fq & 1) << 2);
  unsigned aoff[2], woff[2];
#pragma unroll
  for (int i = 0; i < 2; ++i) {
    const int r8 = lane >> 3, cp8 = lane & 7, sA = (i << 2) | (r8 >> 1);
    aoff[i] = (unsigned)((brow + (w4 * 2 + i) * 8 + r8) * lda + grp * 64 + 8 * (cp8 ^ sA));
    const int k = (w4 * 2 + i) * 4 + (lane >> 4), cp = lane & 15, f = (k & 3) | (((k >> 3) & 1) << 2);
    woff[i] = (unsigned)((grp * 64 + k) * DM + bcol + 8 * (cp ^ (f << 1)));
  }
  LAS char* gA = shm + grp * 81920; LAS char* gW = gA + 49152;
  const int stg = w4 * 2048;
#define STAGE_A3(sa, b, h) do { const size_t o_ = (size_t)((h) * 64) * lda + (b) * 128; \
    glds16(A + (size_t)aoff[0] + o_, gA + (sa) + (h) * 8192 + stg); glds16(A + (size_t)aoff[1] + o_, gA + (sa) + (h) * 8192 + stg + 1024); } while (0)
#define STAGE_W3(ws, c) do { const size_t o_ = (size_t)(((c) >> 1) * 128 + ((c) & 1) * 32) * DM; \
    glds16(Wt + (size_t)woff[0] + o_, gW + (ws) * 8192 + stg); glds16(Wt + (size_t)woff[1] + o_, gW + (ws) * 8192 + stg + 1024); } while (0)
  const int a_s = (fr >> 1) & 7, a_row = grp * 81920 + wr * 8192 + (fr >> 3) * 1024 + (fr & 7) * 128;
  const int a_base0 = a_row + ((fq ^ a_s) << 4), a_base1 = a_row + (((4 + fq) ^ a_s) << 4);
  int w_base[4];
#pragma unroll
  for (int n = 0; n < 4; ++n) w_base[n] = (int)(uintptr_t)shm + grp * 81920 + 49152 + (8 * fq + tq) * 256 + ((((wc << 2) + n) ^ tf) << 5) + 8 * tp;
  f32x4 acc[4][4] = {};
  bf16x8 At[4], Bt[4];
#define MMA3() do { __builtin_amdgcn_s_setprio(1); \
    _Pragma("unroll") for (int m = 0; m < 4; ++m) _Pragma("unroll") for (int n = 0; n < 4; ++n) \
      acc[m][n] = __builtin_amdgcn_mfma_f32_16x16x32_bf16(Bt[n], At[m], acc[m][n], 0, 0, 0); \
    __builtin_amdgcn_s_setprio(0); } while (0)
#define PHASE(WS, KS, sa_rd, STG, WAITS) do { \
    Bt[0] = ld_w<(WS) * 8192>(w_base[0]); Bt[1] = ld_w<(WS) * 8192>(w_base[1]); Bt[2] = ld_w<(WS) * 8192>(w_base[2]); Bt[3] = ld_w<(WS) * 8192>(w_base[3]); \
    { const LAS char* ap_ = shm + ((KS) ? a_base1 : a_base0) + (sa_rd); _Pragma("unroll") for (int m = 0; m < 4; ++m) At[m] = ld_a(ap_ + m * 2048); } \
    STG; BAR; WL0; MMA3(); WAITS; BAR; } while (0)
  const unsigned eoff = (unsigned)((brow + wr * 64 + grp * 32 + fr) * DM + bcol + wc * 64 + (fq & 1) * 16 + (fq >> 1) * 8);
  STAGE_A3(0, 0, 0); STAGE_A3(0, 0, 1); STAGE_W3(0, 0); STAGE_A3(16384, 1, 0); STAGE_W3(1, 1); STAGE_A3(16384, 1, 1); STAGE_W3(2, 2);
  u32x4 xv[4];
#pragma unroll
  for (int q = 0; q < 4; ++q) xv[q] = *reinterpret_cast<const u32x4*>(x + eoff + (q >> 1) * 16 * DM + (q & 1) * 32);
  unsigned tlb0, tlb1;
  asm volatile("global_load_dword %0, %1, %2 sc1" : "=v"(tlb0) : "v"(eoff * 2), "s"(xch) : "memory");
  asm volatile("global_load_dword %0, %1, %2" : "=v"(tlb1) : "v"(eoff * 2), "s"(out) : "memory");
  WAIT_V(14); BAR;
  if (grp == 1) BAR;
  const int tile = pm * 8 + pn;
  unsigned* tick = sync + tile * 64; unsigned* flag = tick + 32 + wid;
  unsigned ticket_v = 0;
  int sA0 = 0, sA1 = 16384, sA2 = 32768;
  for (int p = 0; p < nph - 4; p += 4) {
    const int b0 = p >> 1;
    if (p == nph - 16 && tid == 0)
      asm volatile("global_atomic_add %0, %1, %2, %3 sc0\n\ts_nop 1" : "+v"(ticket_v) : "v"(0u), "v"(1u), "s"(tick) : "memory");
    PHASE(0, 0, sA0, STAGE_A3(sA2, b0 + 2, 0); STAGE_W3(3, p + 3), WAIT_V(8));
    PHASE(1, 1, sA0, STAGE_A3(sA2, b0 + 2, 1); STAGE_W3(0, p + 4), WAIT_V(8));
    PHASE(2, 0, sA1, STAGE_A3(sA0, b0 + 3, 0); STAGE_W3(1, p + 5), WAIT_V(8));
    PHASE(3, 1, sA1, STAGE_A3(sA0, b0 + 3, 1); STAGE_W3(2, p + 6), WAIT_V(8));
    const int t_ = sA0; sA0 = sA2; sA2 = sA1; sA1 = t_;
  }
  PHASE(0, 0, sA0, STAGE_W3(3, nph - 1), WAIT_V(6));
  PHASE(1, 1, sA0, (void)0, WAIT_V(2));
  PHASE(2, 0, sA1, (void)0, WAIT_V(0));
  asm volatile("" :: "v"(tlb0), "v"(tlb1));
  const __amdgpu_buffer_rsrc_t rs = __builtin_amdgcn_make_buffer_rsrc(xch, 0, (int)((size_t)SEQ * DM * 2), 0x00020000);
  volatile LAS unsigned* bc = (volatile LAS unsigned*)(shm + sA2);
  if (wid == 0) { const unsigned tk = __builtin_amdgcn_readfirstlane(ticket_v); if (lane == 0) *bc = tk; WAIT_L(0); }
  BAR; asm volatile("" ::: "memory");
  const unsigned ticket = *bc;
  u32x4 oth[4] = {};
  if (ticket != 0u) {
    const unsigned long long t0 = __builtin_amdgcn_s_memrealtime();
    while (__builtin_amdgcn_readfirstlane(__hip_atomic_load(flag, __ATOMIC_RELAXED, __HIP_MEMORY_SCOPE_AGENT)) == 0u) {
      __builtin_amdgcn_s_sleep(1);
      if (__builtin_amdgcn_s_memrealtime() - t0 > 2000000ull) break;
    }
    asm volatile("" ::: "memory");
#pragma unroll
    for (int q = 0; q < 4; ++q) oth[q] = __builtin_amdgcn_raw_buffer_load_b128(rs, (eoff + (q >> 1) * 16 * DM + (q & 1) * 32) * 2, 0, 16);
  }
  asm volatile("" ::: "memory");
  PHASE(3, 1, sA1, (void)0, (void)0);
  if (grp == 0) BAR;
  {
    LAS f32x4* R = (LAS f32x4*)shm + (w4 * 8) * 64 + lane;
#pragma unroll
    for (int q = 0; q < 8; ++q) R[grp * 2048 + q * 64] = grp ? acc[q >> 2][q & 3] : acc[2 + (q >> 2)][q & 3];
    WAIT_L(0); BAR; asm volatile("" ::: "memory");
#pragma unroll
    for (int q = 0; q < 8; ++q) { const f32x4 v = R[(grp ^ 1) * 2048 + q * 64]; if (grp) acc[2 + (q >> 2)][q & 3] += v; else acc[q >> 2][q & 3] += v; }
  }
  u32x4 own[4];
#pragma unroll
  for (int mm = 0; mm < 2; ++mm)
#pragma unroll
    for (int np = 0; np < 2; ++np) {
      f32x4 v0 = grp ? acc[2 + mm][2 * np] : acc[mm][2 * np], v1 = grp ? acc[2 + mm][2 * np + 1] : acc[mm][2 * np + 1];
      if (role) {
#pragma unroll
        for (int e = 0; e < 4; ++e) { v0[e] = silu_f(rbf(v0[e])); v1[e] = silu_f(rbf(v1[e])); }
      }
      const unsigned a0 = pk2bf(v0[0], v0[1]), a1 = pk2bf(v0[2], v0[3]), b0 = pk2bf(v1[0], v1[1]), b1 = pk2bf(v1[2], v1[3]);
      const auto r0 = __builtin_amdgcn_permlane16_swap(a0, b0, false, false);
      const auto r1 = __builtin_amdgcn_permlane16_swap(a1, b1, false, false);
      own[mm * 2 + np] = (u32x4){r0[0], r1[0], r0[1], r1[1]};
    }
  if (ticket == 0u) {
#pragma unroll
    for (int q = 0; q < 4; ++q) __builtin_amdgcn_raw_buffer_store_b128(own[q], rs, (eoff + (q >> 1) * 16 * DM + (q & 1) * 32) * 2, 0, 16);
    asm volatile("s_waitcnt vmcnt(0)" ::: "memory");
    if (lane == 0) __hip_atomic_store(flag, 1u, __ATOMIC_RELAXED, __HIP_MEMORY_SCOPE_AGENT);
  } else {
#pragma unroll
    for (int q = 0; q < 4; ++q) {
      u32x4 w;
#pragma unroll
      for (int e = 0; e < 4; ++e) {
        const float p0 = rbf(__uint_as_float(own[q][e] << 16) * __uint_as_float(oth[q][e] << 16)), p1 = rbf(__uint_as_float(own[q][e] & 0xffff0000u) * __uint_as_float(oth[q][e] & 0xffff0000u));
        w[e] = pk2bf(__uint_as_float(xv[q][e] << 16) + p0, __uint_as_float(xv[q][e] & 0xffff0000u) + p1);
      }
      *reinterpret_cast<u32x4*>(out + eoff + (q >> 1) * 16 * DM + (q & 1) * 32) = w;
    }
  }
#undef STAGE_A3
#undef STAGE_W3
#undef MMA3
#undef PHASE
}

extern "C" void kernel_launch(void* const* d_in, const int* in_sizes, int n_in, void* d_out, int out_size, void* d_ws, size_t ws_size, hipStream_t stream) {
  const u16* x = (const u16*)d_in[0]; const u16* kc = (const u16*)d_in[1]; const u16* vc = (const u16*)d_in[2];
  const u16* wq = (const u16*)d_in[3]; const u16* wo = (const u16*)d_in[4]; const u16* w1 = (const u16*)d_in[6]; const u16* w2 = (const u16*)d_in[7];
  char* ws = (char*)d_ws;
  u16* xn = (u16*)(ws + WS_XN); u16* H = (u16*)(ws + WS_H); u16* O = (u16*)(ws + WS_O);
  hipFuncSetAttribute((const void*)fused1_kernel, hipFuncAttributeMaxDynamicSharedMemorySize, F_SHM);
  hipFuncSetAttribute((const void*)gemm3_kernel, hipFuncAttributeMaxDynamicSharedMemorySize, G3_SHM);
  unsigned* sync = (unsigned*)(ws + WS_SYNC);
  ln_kernel<<<SEQ / 4, 256, 0, stream>>>(x, xn, sync);
  fused1_kernel<<<256, 512, F_SHM, stream>>>(xn, wq, w1, kc, vc, H, O, wo, w2);
  gemm3_kernel<<<256, 512, G3_SHM, stream>>>(O, H, wo, w2, x, (u16*)d_out, (u16*)(ws + WS_XCH), sync);
}
```
